```python
import torch

def _softmax(x, axis=-1):
    return torch.softmax(x, dim=axis)
def _gelu(x):
    return torch.nn.functional.gelu(x, approximate="tanh")
def _silu(x):
    return torch.nn.functional.silu(x)
def _sigmoid(x):
    return torch.sigmoid(x)
def _layernorm(x, axis=-1, eps=1e-5):
    m = x.mean(dim=axis, keepdim=True)
    v = x.var(dim=axis, keepdim=True, unbiased=False)
    return (x - m) / torch.sqrt(v + eps)
def _scatter_add(tgt, idx, src, axis=0):
    return tgt.clone().index_add_(axis, idx, src)
def _segment_sum(src, seg, n):
    out = torch.zeros((n,) + src.shape[1:], dtype=src.dtype)
    return out.index_add_(0, seg, src)
def _pad(x, pw):
    flat = tuple(v for pair in reversed(pw) for v in pair)
    return torch.nn.functional.pad(x, flat)

kernel_name = 'parallel_sum__sliding_block_cached__glu_gated_swiglu__decode'

def setup_inputs():

    _w = (lambda a: (lambda t: t.to(torch.bfloat16) if t.is_floating_point() else t)(torch.as_tensor(a)))
    def _A_setup():
        B, Sq, D = 1, 1, 512
        Hq, Hkv, Sk, Dh = 32, 8, 65536, 128
        assert Hq % Hkv == 0, (Hq, Hkv)
        return {
            "x": _w((lambda *s: torch.randn(*s, dtype=torch.bfloat16) * 0.05)(B, Sq, D)),
            "k_cache": _w((lambda *s: torch.randn(*s, dtype=torch.bfloat16) * 1.6)(B, Hkv, Sk, Dh)),
            "v_cache": _w((lambda *s: torch.randn(*s, dtype=torch.bfloat16) * 0.8)(B, Hkv, Sk, Dh)),
            "wq": _w((lambda *s: torch.randn(*s, dtype=torch.bfloat16) * 1.6)(D, Hq * Dh) * (D ** -0.5)),
            "wo": _w((lambda *s: torch.randn(*s, dtype=torch.bfloat16) * 0.8)(Hq * Dh, D) * ((Hq * Dh) ** -0.5)),
        }
    _A_in = _A_setup()

    def _B_setup():
        B, S, D, F = 1, 1, 512, 1792
        return {
            "x": _w((lambda *s: torch.randn(*s, dtype=torch.bfloat16) * 0.05)(B, S, D)),
            "w1": _w((lambda *s: torch.randn(*s, dtype=torch.bfloat16) * 0.4)(D, F) * (2.0 / D) ** 0.5),
            "w3": _w((lambda *s: torch.randn(*s, dtype=torch.bfloat16) * 0.4)(D, F) * (2.0 / D) ** 0.5),
            "w2": _w((lambda *s: torch.randn(*s, dtype=torch.bfloat16) * 0.4)(F, D) * (2.0 / F) ** 0.5),
        }
    _B_in = _B_setup()

    return {
        "A_x": _A_in["x"],
        "A_k_cache": _A_in["k_cache"],
        "A_v_cache": _A_in["v_cache"],
        "A_wq": _A_in["wq"],
        "A_wo": _A_in["wo"],
        "B_x": _B_in["x"],
        "B_w1": _B_in["w1"],
        "B_w3": _B_in["w3"],
        "B_w2": _B_in["w2"],
    }

def reference(A_x, A_k_cache, A_v_cache, A_wq, A_wo, B_x, B_w1, B_w3, B_w2):
    def _A_ref(x, k_cache, v_cache, wq, wo):
        B, Sq, D = x.shape
        Hq, Dh = 32, 128
        Hkv, Sk = k_cache.shape[1], k_cache.shape[2]
        W = min(4096, max(256, Sk // 4))
        rep = Hq // Hkv
        q = (lambda x, *d: x.permute(*d))((x @ wq).reshape(B, Sq, Hq, Dh), 0, 2, 1, 3)
        k = (lambda x, n, axis: torch.repeat_interleave(x, n, dim=axis))(k_cache[:, :, Sk - W:, :], rep, axis=1)
        v = (lambda x, n, axis: torch.repeat_interleave(x, n, dim=axis))(v_cache[:, :, Sk - W:, :], rep, axis=1)
        scores = torch.einsum("bhqd,bhkd->bhqk", q, k) * (Dh ** -0.5)
        p = _softmax(scores, axis=-1)
        o = torch.einsum("bhqk,bhkd->bhqd", p, v)
        return (lambda x, *d: x.permute(*d))(o, 0, 2, 1, 3).reshape(B, Sq, Hq * Dh) @ wo

    def _B_ref(x, w1, w3, w2):
        gate = torch.einsum("bsd,df->bsf", x, w1)
        up = torch.einsum("bsd,df->bsf", x, w3)
        h = _silu(gate) * up
        out = torch.einsum("bsf,fd->bsd", h, w2)
        return out

    x = A_x
    xn = _layernorm(x, axis=-1)
    return x + _A_ref(xn, A_k_cache, A_v_cache, A_wq, A_wo) + _B_ref(xn, B_w1, B_w3, B_w2)
```

```cpp
#include <hip/hip_runtime.h>
#include <hip/hip_bf16.h>
#include <stdint.h>
typedef unsigned short bf16_t;
typedef unsigned u32x4 __attribute__((ext_vector_type(4)));
__device__ __forceinline__ float bflo(unsigned u){ return __uint_as_float(u<<16);}
__device__ __forceinline__ float bfhi(unsigned u){ return __uint_as_float(u&0xffff0000u);}
__device__ __forceinline__ float bf2f(bf16_t b){ return __uint_as_float(((unsigned)b)<<16);}
constexpr int D=512, HQ=32, HKV=8, SK=65536, DH=128, W=4096, F=1792;

__device__ void layernorm(const bf16_t* x, float* xn, float* red){
  int t=threadIdx.x;
  float a=bf2f(x[t]), b=bf2f(x[t+256]);
  float s=a+b;
  for(int o=32;o;o>>=1) s+=__shfl_xor(s,o);
  if((t&63)==0) red[t>>6]=s;
  __syncthreads();
  float m=(red[0]+red[1]+red[2]+red[3])*(1.f/512.f);
  __syncthreads();
  float da=a-m, db=b-m; float v=da*da+db*db;
  for(int o=32;o;o>>=1) v+=__shfl_xor(v,o);
  if((t&63)==0) red[t>>6]=v;
  __syncthreads();
  float var=(red[0]+red[1]+red[2]+red[3])*(1.f/512.f);
  float r=rsqrtf(var+1e-5f);
  xn[t]=da*r; xn[t+256]=db*r;
  __syncthreads();
}
__global__ void __launch_bounds__(256) k1(const bf16_t* x,const bf16_t* wq,const bf16_t* w1,const bf16_t* w3,float* q,float* h){
  __shared__ float xn[512]; __shared__ float red[4]; __shared__ float part[2][64][33];
  layernorm(x,xn,red);
  int t=threadIdx.x, c=t&3, r=t>>2;
  int b=blockIdx.x;
  float acc[8]={0}, acc2[8]={0};
  if(b<128){
    for(int i=0;i<8;i++){ int row=r+64*i; u32x4 w=*(const u32x4*)(wq+(size_t)row*4096+b*32+c*8); float xv=xn[row];
      for(int k=0;k<4;k++){ acc[2*k]+=xv*bflo(w[k]); acc[2*k+1]+=xv*bfhi(w[k]); } }
  } else { int bb=b-128;
    for(int i=0;i<8;i++){ int row=r+64*i; u32x4 w=*(const u32x4*)(w1+(size_t)row*F+bb*32+c*8); u32x4 v=*(const u32x4*)(w3+(size_t)row*F+bb*32+c*8); float xv=xn[row];
      for(int k=0;k<4;k++){ acc[2*k]+=xv*bflo(w[k]); acc[2*k+1]+=xv*bfhi(w[k]); acc2[2*k]+=xv*bflo(v[k]); acc2[2*k+1]+=xv*bfhi(v[k]); } }
  }
  for(int k=0;k<8;k++){ part[0][r][c*8+k]=acc[k]; part[1][r][c*8+k]=acc2[k]; }
  __syncthreads();
  if(t<32){ float s=0,s2=0; for(int i=0;i<64;i++){ s+=part[0][i][t]; s2+=part[1][i][t]; }
    if(b<128) q[b*32+t]=s; else { float g=s; h[(b-128)*32+t]=g/(1.f+__expf(-g))*s2; } }
}
__global__ void __launch_bounds__(256) k2(const bf16_t* kc,const bf16_t* vc,const float* q,float* po,float* pml){
  __shared__ float qs[4][128]; __shared__ float sc[4][128]; __shared__ float ml[4][2]; __shared__ float op[2][4][128];
  int b=blockIdx.x,g=b&7,j=b>>3,t=threadIdx.x;
  for(int i=t;i<512;i+=256) qs[i>>7][i&127]=q[g*512+i]*0.08838834764f;
  __syncthreads();
  const bf16_t* kb=kc+((size_t)g*SK+(SK-W)+j*128)*DH; const bf16_t* vb=vc+((size_t)g*SK+(SK-W)+j*128)*DH;
  int c=t&15, r=t>>4;
  for(int i=0;i<8;i++){ int key=r+16*i; u32x4 w=*(const u32x4*)(kb+key*DH+c*8);
    float s[4]; for(int hh=0;hh<4;hh++){ float a=0; for(int k=0;k<4;k++){ a+=qs[hh][c*8+2*k]*bflo(w[k])+qs[hh][c*8+2*k+1]*bfhi(w[k]); }
      for(int o=8;o;o>>=1) a+=__shfl_xor(a,o); s[hh]=a; }
    if(c==0) for(int hh=0;hh<4;hh++) sc[hh][key]=s[hh]; }
  __syncthreads();
  { int hh=t>>6, l=t&63; float a=sc[hh][l], bb=sc[hh][l+64]; float m=fmaxf(a,bb); for(int o=32;o;o>>=1) m=fmaxf(m,__shfl_xor(m,o));
    a=__expf(a-m); bb=__expf(bb-m); float s=a+bb; for(int o=32;o;o>>=1) s+=__shfl_xor(s,o); sc[hh][l]=a; sc[hh][l+64]=bb; if(l==0){ ml[hh][0]=m; ml[hh][1]=s; } }
  __syncthreads();
  { int d=t&127, half=t>>7; float a[4]={0,0,0,0}; for(int k=half*64;k<half*64+64;k++){ float v=bf2f(vb[k*DH+d]); for(int hh=0;hh<4;hh++) a[hh]+=sc[hh][k]*v; }
    for(int hh=0;hh<4;hh++) op[half][hh][d]=a[hh]; }
  __syncthreads();
  for(int i=t;i<512;i+=256){ int hh=i>>7,d=i&127; po[((size_t)(g*4+hh)*32+j)*128+d]=op[0][hh][d]+op[1][hh][d]; }
  if(t<4){ pml[((g*4+t)*32+j)*2]=ml[t][0]; pml[((g*4+t)*32+j)*2+1]=ml[t][1]; }
}
__global__ void __launch_bounds__(256) k3(const float* po,const float* pml,const float* h,const bf16_t* wo,const bf16_t* w2,float* part){
  __shared__ float v[128]; __shared__ float red[4][512];
  int b=blockIdx.x,t=threadIdx.x; const bf16_t* wb;
  if(b<32){ if(t<128){ float M=-INFINITY; for(int s=0;s<32;s++) M=fmaxf(M,pml[(b*32+s)*2]); float L=0,o=0; for(int s=0;s<32;s++){ float w=__expf(pml[(b*32+s)*2]-M); L+=w*pml[(b*32+s)*2+1]; o+=w*po[((size_t)b*32+s)*128+t]; } v[t]=o/L; } wb=wo+(size_t)b*128*512; }
  else { if(t<128) v[t]=h[(b-32)*128+t]; wb=w2+(size_t)(b-32)*128*512; }
  __syncthreads();
  int c=t&63, r=t>>6; float acc[8]={0};
  for(int i=0;i<32;i++){ int row=r+4*i; u32x4 w=*(const u32x4*)(wb+row*512+c*8); float xv=v[row]; for(int k=0;k<4;k++){ acc[2*k]+=xv*bflo(w[k]); acc[2*k+1]+=xv*bfhi(w[k]); } }
  for(int k=0;k<8;k++) red[r][c*8+k]=acc[k];
  __syncthreads();
  for(int i=t;i<512;i+=256) part[b*512+i]=red[0][i]+red[1][i]+red[2][i]+red[3][i];
}
__global__ void k4(const float* part,const bf16_t* x,__hip_bfloat16* out){ int t=blockIdx.x*64+threadIdx.x; float s=bf2f(x[t]); for(int b=0;b<46;b++) s+=part[b*512+t]; out[t]=__float2bfloat16(s); }
extern "C" void kernel_launch(void* const* d_in, const int* in_sizes, int n_in, void* d_out, int out_size, void* d_ws, size_t ws_size, hipStream_t stream){
  const bf16_t* x=(const bf16_t*)d_in[0]; const bf16_t* kc=(const bf16_t*)d_in[1]; const bf16_t* vc=(const bf16_t*)d_in[2];
  const bf16_t* wq=(const bf16_t*)d_in[3]; const bf16_t* wo=(const bf16_t*)d_in[4]; const bf16_t* w1=(const bf16_t*)d_in[6]; const bf16_t* w3=(const bf16_t*)d_in[7]; const bf16_t* w2=(const bf16_t*)d_in[8];
  float* ws=(float*)d_ws; float* q=ws; float* h=ws+4096; float* pml=ws+8192; float* po=ws+16384; float* part=po+32*32*128;
  k1<<<184,256,0,stream>>>(x,wq,w1,w3,q,h);
  k2<<<256,256,0,stream>>>(kc,vc,q,po,pml);
  k3<<<46,256,0,stream>>>(po,pml,h,wo,w2,part);
  k4<<<8,64,0,stream>>>(part,x,(__hip_bfloat16*)d_out);
}
```

```cpp
#include <hip/hip_runtime.h>
#include <stdint.h>

typedef unsigned u32;
typedef unsigned long long u64;
typedef unsigned short bf16_t;
typedef u32 u32x2 __attribute__((ext_vector_type(2)));
typedef u32 u32x4 __attribute__((ext_vector_type(4)));
typedef float f32x2 __attribute__((ext_vector_type(2)));
typedef float f32x4 __attribute__((ext_vector_type(4)));
typedef short s16x4 __attribute__((ext_vector_type(4)));
typedef __bf16 bf16x2 __attribute__((ext_vector_type(2)));
typedef __bf16 bf16x8 __attribute__((ext_vector_type(8)));
#define GAS __attribute__((address_space(1)))
#define RLX_AGENT __ATOMIC_RELAXED, __HIP_MEMORY_SCOPE_AGENT

#define DIAG 0
#define NT_KV 1
#define NT_WO 1

constexpr int D = 512, NQ = 4096, SK = 65536, WIN = 4096, DH = 128, F = 1792;
constexpr int NWG = 256, NTHR = 512;
constexpr u32 WS_EPOCH = 0;
constexpr u32 WS_Q = 4096;
constexpr u32 WS_ML = WS_Q + 16384;
constexpr u32 WS_A = 65536;
constexpr u32 WS_O = WS_A + (1u << 20);
constexpr u32 WS_DIAG = WS_O + (1u << 20);
constexpr u32 WS_END = WS_DIAG + 65536;
constexpr u32 SPIN_CAP = 200000;

__device__ __forceinline__ float bflo(u32 u) { return __uint_as_float(u << 16); }
__device__ __forceinline__ float bfhi(u32 u) { return __uint_as_float(u & 0xffff0000u); }
__device__ __forceinline__ u32 pk_bf16(float lo, float hi) {
  bf16x2 v; v[0] = (__bf16)lo; v[1] = (__bf16)hi; return __builtin_bit_cast(u32, v);
}
__device__ __forceinline__ float rbf(float x) { return (float)(__bf16)x; }
__device__ __forceinline__ float dot2(u32 a, u32 b, float acc) {
  return __builtin_amdgcn_fdot2_f32_bf16(__builtin_bit_cast(bf16x2, a), __builtin_bit_cast(bf16x2, b), acc, false);
}
template <int CTRL> __device__ __forceinline__ float dpp(float x) {
  return __builtin_bit_cast(float, __builtin_amdgcn_mov_dpp(__builtin_bit_cast(int, x), CTRL, 0xf, 0xf, true));
}
template <int CTRL> __device__ __forceinline__ u32 dppu(u32 x) { return (u32)__builtin_amdgcn_mov_dpp((int)x, CTRL, 0xf, 0xf, true); }
constexpr int XOR1 = 0xB1, XOR2 = 0x4E, XOR7 = 0x141, XOR8 = 0x128;
__device__ __forceinline__ float swap32_add(float a, float b) {
  auto s = __builtin_amdgcn_permlane32_swap(__float_as_uint(a), __float_as_uint(b), false, false);
  return __uint_as_float(s[0]) + __uint_as_float(s[1]);
}
__device__ __forceinline__ float swap16_add(float a, float b) {
  auto s = __builtin_amdgcn_permlane16_swap(__float_as_uint(a), __float_as_uint(b), false, false);
  return __uint_as_float(s[0]) + __uint_as_float(s[1]);
}
__device__ __forceinline__ float swap32_max(float a) {
  auto s = __builtin_amdgcn_permlane32_swap(__float_as_uint(a), __float_as_uint(a), false, false);
  return fmaxf(__uint_as_float(s[0]), __uint_as_float(s[1]));
}
__device__ __forceinline__ float swap16_max(float a) {
  auto s = __builtin_amdgcn_permlane16_swap(__float_as_uint(a), __float_as_uint(a), false, false);
  return fmaxf(__uint_as_float(s[0]), __uint_as_float(s[1]));
}
__device__ __forceinline__ float wave_sum(float v) {
  v += dpp<XOR1>(v); v += dpp<XOR2>(v); v += dpp<XOR7>(v); v += dpp<XOR8>(v);
  v = swap16_add(v, v); v = swap32_add(v, v);
  return v;
}
__device__ __forceinline__ float rows2_reduce(u32x4 a, u32x4 b, u32 xp, int lane) {
  float v[8];
#pragma unroll
  for (int k = 0; k < 4; ++k) {
    v[2 * k] = dot2(__builtin_amdgcn_perm(b[k], a[k], 0x05040100u), xp, 0.f);
    v[2 * k + 1] = dot2(__builtin_amdgcn_perm(b[k], a[k], 0x07060302u), xp, 0.f);
  }
  const float u0 = swap32_add(v[0], v[1]), u1 = swap32_add(v[2], v[3]), u2 = swap32_add(v[4], v[5]), u3 = swap32_add(v[6], v[7]);
  float t0 = swap16_add(u0, u1), t1 = swap16_add(u2, u3);
  t0 += dpp<XOR8>(t0); t1 += dpp<XOR8>(t1);
  float t = (lane & 8) ? t1 : t0;
  t += dpp<XOR7>(t); t += dpp<XOR1>(t); t += dpp<XOR2>(t);
  return t;
}
__device__ __forceinline__ void wg_barrier() {
  asm volatile("s_waitcnt lgkmcnt(0)" ::: "memory");
  __builtin_amdgcn_s_barrier();
  asm volatile("" ::: "memory");
}
template <bool NT> __device__ __forceinline__ u32x4 ld16(const bf16_t* p) {
  if constexpr (NT) return __builtin_nontemporal_load((const GAS u32x4*)p); else return *(const GAS u32x4*)p;
}
template <bool NT> __device__ __forceinline__ u32 ld4(const bf16_t* p) {
  if constexpr (NT) return __builtin_nontemporal_load((const GAS u32*)p); else return *(const GAS u32*)p;
}

#define STAMP(i) do { } while (0)

struct Params {
  const bf16_t* x; const bf16_t* kc; const bf16_t* vc; const bf16_t* wq; const bf16_t* wo;
  const bf16_t* w1; const bf16_t* w3; const bf16_t* w2; bf16_t* out; unsigned char* ws;
};

__global__ void __launch_bounds__(NTHR, 2) fused_decode(Params p) {
  __shared__ __attribute__((aligned(16))) float s_part[8][8];
  __shared__ __attribute__((aligned(16))) float s_fpart[8][8];
  __shared__ __attribute__((aligned(16))) u32 s_q[256];
  __shared__ __attribute__((aligned(16))) float s_o[8][512];
  __shared__ __attribute__((aligned(16))) float s_m[4][8], s_l[4][8];
  __shared__ __attribute__((aligned(16))) u32 s_op[8];
  __shared__ __attribute__((aligned(16))) float s_fin[8][4];

  const int tid = threadIdx.x, lane = tid & 63, wave = __builtin_amdgcn_readfirstlane(tid >> 6);
  const int b = blockIdx.x, g = b & 7, j = b >> 3;
  const int r = lane >> 4, c = lane & 15;
  unsigned char* ws = p.ws;
  STAMP(0);
  const __amdgpu_buffer_rsrc_t rs = __builtin_amdgcn_make_buffer_rsrc(ws, 0, WS_END, 0x00020000);

  const u32x4 xv = ld16<false>(p.x + 8 * lane);
  const u32 tag = __hip_atomic_load((GAS u32*)(ws + WS_EPOCH), RLX_AGENT) + 1u;
  const int hf = wave & 1, u = wave >> 1;
  const int row0 = 8 * lane + 2 * u;
  const bf16_t* wqp = p.wq + (size_t)row0 * NQ + g * 512 + j * 16 + hf * 8;
  const u32x4 wqa = ld16<false>(wqp), wqb = ld16<false>(wqp + NQ);
  const size_t kvoff = ((size_t)g * SK + (SK - WIN) + j * 128 + wave * 16) * DH;
  u32x4 kf[4], vf[4];
#pragma unroll
  for (int s = 0; s < 4; ++s) kf[s] = ld16<NT_KV>(p.kc + kvoff + c * DH + (4 * s + r) * 8);
#pragma unroll
  for (int i = 0; i < 4; ++i) vf[i] = ld16<NT_KV>(p.vc + kvoff + (4 * r + i) * DH + c * 8);
  __builtin_amdgcn_sched_barrier(0);

  float xf[8];
#pragma unroll
  for (int k = 0; k < 4; ++k) { xf[2 * k] = bflo(xv[k]); xf[2 * k + 1] = bfhi(xv[k]); }
  float sum = ((xf[0] + xf[1]) + (xf[2] + xf[3])) + ((xf[4] + xf[5]) + (xf[6] + xf[7]));
  const float mean = wave_sum(sum) * (1.f / D);
  float sq = 0.f;
#pragma unroll
  for (int k = 0; k < 8; ++k) { const float d = xf[k] - mean; sq = fmaf(d, d, sq); }
  const float var = wave_sum(sq) * (1.f / D);
  const float mb = rbf(mean), sd = rbf(__builtin_sqrtf(rbf(rbf(var) + 1e-5f)));
  const u32 xd = u == 0 ? xv[0] : u == 1 ? xv[1] : u == 2 ? xv[2] : xv[3];
  const u32 xp = pk_bf16(rbf(bflo(xd) - mb) / sd, rbf(bfhi(xd) - mb) / sd);
  STAMP(1);

  {
    const float t = rows2_reduce(wqa, wqb, xp, lane);
    const int idx = ((r & 1) << 1 | (r >> 1)) + 4 * ((lane >> 3) & 1);
    if ((lane & 7) == 0) s_part[wave][idx] = t;
  }
  wg_barrier();
  if (wave == 0 && lane < 8) {
    const int h2 = lane >> 2, k2 = (lane & 3) * 2;
    float a0 = 0.f, a1 = 0.f;
#pragma unroll
    for (int uu = 0; uu < 4; ++uu) { a0 += s_part[2 * uu + h2][k2]; a1 += s_part[2 * uu + h2][k2 + 1]; }
    const u64 gr = ((u64)tag << 32) | pk_bf16(a0, a1);
    __hip_atomic_store((GAS u64*)(ws + WS_Q) + (g * 32 + j) * 8 + lane, gr, RLX_AGENT);
  }
  STAMP(2);

  asm volatile("" :: "v"(kf[0]), "v"(kf[1]), "v"(kf[2]), "v"(kf[3]));
  __builtin_amdgcn_sched_barrier(0);
  STAMP(3);
  u32x4 qg0, qg1;
  if (wave == 0) {
    qg0 = __builtin_amdgcn_raw_buffer_load_b128(rs, WS_Q + g * 2048 + lane * 32, 0, 16);
    qg1 = __builtin_amdgcn_raw_buffer_load_b128(rs, WS_Q + g * 2048 + lane * 32 + 16, 0, 16);
  }
  __builtin_amdgcn_sched_barrier(0);
  const int fline = g + 8 * (j >> 3);
  const bool has_ffn = fline < 28;
  const int fcol0 = has_ffn ? fline * 64 + (j & 7) * 8 : 0;
  const bf16_t* wfp = (hf ? p.w3 : p.w1) + (size_t)row0 * F + fcol0;
  const u32x4 wfa = ld16<false>(wfp), wfb = ld16<false>(wfp + F);
  u32 wor[16], w2r[8];
  if (wave >= 4) {
    const int t2 = 2 * (tid - 256);
#pragma unroll
    for (int i = 0; i < 8; ++i) w2r[i] = ld4<NT_WO>(p.w2 + (size_t)(fcol0 + i) * D + t2);
#pragma unroll
    for (int i = 0; i < 16; ++i) wor[i] = ld4<NT_WO>(p.wo + (size_t)(g * 512 + j * 16 + i) * D + t2);
  }
  __builtin_amdgcn_sched_barrier(0);
  if (wave == 0) {
    u32 spins = 0;
    while (!__all(qg0[1] == tag && qg0[3] == tag && qg1[1] == tag && qg1[3] == tag)) {
      if (++spins > SPIN_CAP) break;
      __builtin_amdgcn_s_sleep(1);
      asm volatile("" ::: "memory");
      qg0 = __builtin_amdgcn_raw_buffer_load_b128(rs, WS_Q + g * 2048 + lane * 32, 0, 16);
      qg1 = __builtin_amdgcn_raw_buffer_load_b128(rs, WS_Q + g * 2048 + lane * 32 + 16, 0, 16);
    }
    *(u32x4*)&s_q[4 * lane] = u32x4{qg0[0], qg0[2], qg1[0], qg1[2]};
  }
  u32x2 vt[8];
#pragma unroll
  for (int k = 0; k < 4; ++k) {
    vt[2 * k][0] = __builtin_amdgcn_perm(vf[1][k], vf[0][k], 0x05040100u);
    vt[2 * k][1] = __builtin_amdgcn_perm(vf[3][k], vf[2][k], 0x05040100u);
    vt[2 * k + 1][0] = __builtin_amdgcn_perm(vf[1][k], vf[0][k], 0x07060302u);
    vt[2 * k + 1][1] = __builtin_amdgcn_perm(vf[3][k], vf[2][k], 0x07060302u);
  }
  STAMP(4);
  wg_barrier();
  STAMP(5);

  {
    f32x4 sc = {0.f, 0.f, 0.f, 0.f};
#pragma unroll
    for (int s = 0; s < 4; ++s) {
      const u32x4 qf = *(const u32x4*)&s_q[(c & 3) * 64 + s * 16 + r * 4];
      sc = __builtin_amdgcn_mfma_f32_16x16x32_bf16(__builtin_bit_cast(bf16x8, kf[s]), __builtin_bit_cast(bf16x8, qf), sc, 0, 0, 0);
    }
    float t[4];
#pragma unroll
    for (int k = 0; k < 4; ++k) t[k] = rbf(rbf(sc[k]) * 0.08838834764831845f) * 1.4426950408889634f;
    float m = fmaxf(fmaxf(t[0], t[1]), fmaxf(t[2], t[3]));
    m = swap16_max(m); m = swap32_max(m);
    float pe[4];
#pragma unroll
    for (int k = 0; k < 4; ++k) pe[k] = __builtin_amdgcn_exp2f(t[k] - m);
    float l = (pe[0] + pe[1]) + (pe[2] + pe[3]);
    l = swap16_add(l, l); l = swap32_add(l, l);
    const u32 p01 = pk_bf16(pe[0], pe[1]), p23 = pk_bf16(pe[2], pe[3]);
    f32x4 o0 = {0.f, 0.f, 0.f, 0.f}, o1 = {0.f, 0.f, 0.f, 0.f};
#pragma unroll
    for (int e = 0; e < 4; ++e) {
      const bool on = (c >> 2) == e;
      const u32x2 a = {on ? p01 : 0u, on ? p23 : 0u};
      o0 = __builtin_amdgcn_mfma_f32_16x16x16bf16_1k(__builtin_bit_cast(s16x4, a), __builtin_bit_cast(s16x4, vt[2 * e]), o0, 0, 0, 0);
      o1 = __builtin_amdgcn_mfma_f32_16x16x16bf16_1k(__builtin_bit_cast(s16x4, a), __builtin_bit_cast(s16x4, vt[2 * e + 1]), o1, 0, 0, 0);
    }
#pragma unroll
    for (int h = 0; h < 4; ++h) *(f32x2*)&s_o[wave][h * 128 + 8 * c + 2 * r] = f32x2{o0[h], o1[h]};
    if (lane < 4) { s_m[lane][wave] = m; s_l[lane][wave] = l; }
  }
  wg_barrier();
  {
    const int h = wave >> 1;
    const f32x4 ma = *(const f32x4*)&s_m[h][0], mb4 = *(const f32x4*)&s_m[h][4];
    const f32x4 la = *(const f32x4*)&s_l[h][0], lb = *(const f32x4*)&s_l[h][4];
    const float M = fmaxf(fmaxf(fmaxf(ma[0], ma[1]), fmaxf(ma[2], ma[3])), fmaxf(fmaxf(mb4[0], mb4[1]), fmaxf(mb4[2], mb4[3])));
    float o = 0.f, L = 0.f;
#pragma unroll
    for (int w = 0; w < 8; ++w) {
      const float wt = __builtin_amdgcn_exp2f((w < 4 ? ma[w & 3] : mb4[w & 3]) - M);
      o = fmaf(wt, s_o[w][tid], o);
      L = fmaf(wt, (w < 4 ? la[w & 3] : lb[w & 3]), L);
    }
    __hip_atomic_store((GAS u64*)(ws + WS_A) + (size_t)(g * 32 + j) * 512 + tid, ((u64)tag << 32) | __float_as_uint(o), RLX_AGENT);
    if ((tid & 127) == 0)
      __builtin_amdgcn_raw_buffer_store_b128(u32x4{__float_as_uint(M), tag, __float_as_uint(L), tag}, rs, WS_ML + ((g * 4 + h) * 32 + j) * 16, 0, 16);
  }
  STAMP(6);

  {
    const float t = rows2_reduce(wfa, wfb, xp, lane);
    const int idx = ((r & 1) << 1 | (r >> 1)) + 4 * ((lane >> 3) & 1);
    if ((lane & 7) == 0) s_fpart[wave][idx] = t;
  }
  STAMP(7);
  wg_barrier();
  float acc0 = 0.f, acc1 = 0.f;
  u32 wp0[8], wp1[8];
  if (wave == 0) {
    const int r8 = lane >> 3, c8 = lane & 7;
    u32x4 av[4], ml[4];
    u32 spins = 0;
    for (;;) {
      bool ok = true;
#pragma unroll
      for (int t = 0; t < 4; ++t) {
        av[t] = __builtin_amdgcn_raw_buffer_load_b128(rs, WS_A + ((g * 32 + 8 * t + r8) * 512 + j * 16 + 2 * c8) * 8, 0, 16);
        ml[t] = __builtin_amdgcn_raw_buffer_load_b128(rs, WS_ML + ((g * 4 + (j >> 3)) * 32 + 8 * t + r8) * 16, 0, 16);
      }
#pragma unroll
      for (int t = 0; t < 4; ++t) ok &= av[t][1] == tag && av[t][3] == tag && ml[t][1] == tag && ml[t][3] == tag;
      if (__all(ok)) break;
      if (++spins > SPIN_CAP) break;
      __builtin_amdgcn_s_sleep(1);
      asm volatile("" ::: "memory");
    }
    float M = fmaxf(fmaxf(__uint_as_float(ml[0][0]), __uint_as_float(ml[1][0])), fmaxf(__uint_as_float(ml[2][0]), __uint_as_float(ml[3][0])));
    M = fmaxf(M, dpp<XOR8>(M)); M = swap16_max(M); M = swap32_max(M);
    float L = 0.f, a0 = 0.f, a1 = 0.f;
#pragma unroll
    for (int t = 0; t < 4; ++t) {
      const float wt = __builtin_amdgcn_exp2f(__uint_as_float(ml[t][0]) - M);
      L = fmaf(wt, __uint_as_float(ml[t][2]), L);
      a0 = fmaf(wt, __uint_as_float(av[t][0]), a0);
      a1 = fmaf(wt, __uint_as_float(av[t][2]), a1);
    }
    L += dpp<XOR8>(L); a0 += dpp<XOR8>(a0); a1 += dpp<XOR8>(a1);
    L = swap16_add(L, L); a0 = swap16_add(a0, a0); a1 = swap16_add(a1, a1);
    L = swap32_add(L, L); a0 = swap32_add(a0, a0); a1 = swap32_add(a1, a1);
    const float inv = 1.f / L;
    if (lane < 8) s_op[lane] = pk_bf16(a0 * inv, a1 * inv);
  } else if (wave >= 4) {
    const int i8 = lane & 7;
    float ga = 0.f, up = 0.f;
#pragma unroll
    for (int uu = 0; uu < 4; ++uu) { ga += s_fpart[2 * uu][i8]; up += s_fpart[2 * uu + 1][i8]; }
    ga = rbf(ga); up = rbf(up);
    float hv = rbf(ga / (1.f + __expf(-ga))) * up;
    hv = has_ffn ? hv : 0.f;
    const u32 hp = pk_bf16(hv, dpp<XOR1>(hv));
#pragma unroll
    for (int k = 0; k < 4; ++k) {
      const u32 hh = __builtin_amdgcn_readlane(hp, 2 * k);
      acc0 = dot2(__builtin_amdgcn_perm(w2r[2 * k + 1], w2r[2 * k], 0x05040100u), hh, acc0);
      acc1 = dot2(__builtin_amdgcn_perm(w2r[2 * k + 1], w2r[2 * k], 0x07060302u), hh, acc1);
    }
#pragma unroll
    for (int k = 0; k < 8; ++k) {
      wp0[k] = __builtin_amdgcn_perm(wor[2 * k + 1], wor[2 * k], 0x05040100u);
      wp1[k] = __builtin_amdgcn_perm(wor[2 * k + 1], wor[2 * k], 0x07060302u);
    }
  }
  STAMP(8);
  wg_barrier();
  if (wave >= 4) {
    const u32x4 oa = *(const u32x4*)&s_op[0], ob = *(const u32x4*)&s_op[4];
#pragma unroll
    for (int k = 0; k < 8; ++k) {
      const u32 op = k < 4 ? oa[k & 3] : ob[k & 3];
      acc0 = dot2(wp0[k], op, acc0); acc1 = dot2(wp1[k], op, acc1);
    }
    __builtin_amdgcn_raw_buffer_store_b128(u32x4{__float_as_uint(acc0), tag, __float_as_uint(acc1), tag}, rs, WS_O + b * 4096 + (tid - 256) * 16, 0, 16);
  }
  STAMP(9);

  if (j >= 16) return;
  {
    const int col0 = (g + 8 * (j >> 2)) * 16 + (j & 3) * 4;
    const u32 off = WS_O + (tid >> 1) * 4096 + (col0 + 2 * (tid & 1)) * 8;
    u32x4 ov;
    u32 spins = 0;
    for (;;) {
      ov = __builtin_amdgcn_raw_buffer_load_b128(rs, off, 0, 16);
      if (__all(ov[1] == tag && ov[3] == tag)) break;
      if (++spins > SPIN_CAP) break;
      __builtin_amdgcn_s_sleep(1);
      asm volatile("" ::: "memory");
    }
    STAMP(10);
    float s = swap32_add(__uint_as_float(ov[0]), __uint_as_float(ov[2]));
    s = swap16_add(s, s);
    s += dpp<XOR8>(s); s += dpp<XOR2>(s);
    s += dpp<0x124>(s);
    if ((lane & 30) == 0) s_fin[wave][2 * (lane & 1) + (lane >> 5)] = s;
    wg_barrier();
    if (tid < 2) {
      float a0 = 0.f, a1 = 0.f;
#pragma unroll
      for (int w = 0; w < 8; ++w) { a0 += s_fin[w][2 * tid]; a1 += s_fin[w][2 * tid + 1]; }
      const u32 xx = *(const GAS u32*)(p.x + col0 + 2 * tid);
      *(GAS u32*)(p.out + col0 + 2 * tid) = pk_bf16(bflo(xx) + a0, bfhi(xx) + a1);
      if (b == 0 && tid == 0) __hip_atomic_store((GAS u32*)(ws + WS_EPOCH), tag, RLX_AGENT);
    }
    STAMP(11);
  }
}

extern "C" void kernel_launch(void* const* d_in, const int* in_sizes, int n_in, void* d_out, int out_size,
                              void* d_ws, size_t ws_size, hipStream_t stream) {
  Params p;
  p.x = (const bf16_t*)d_in[0]; p.kc = (const bf16_t*)d_in[1]; p.vc = (const bf16_t*)d_in[2];
  p.wq = (const bf16_t*)d_in[3]; p.wo = (const bf16_t*)d_in[4];
  p.w1 = (const bf16_t*)d_in[6]; p.w3 = (const bf16_t*)d_in[7]; p.w2 = (const bf16_t*)d_in[8];
  p.out = (bf16_t*)d_out; p.ws = (unsigned char*)d_ws;
  hipLaunchKernelGGL(fused_decode, dim3(NWG), dim3(NTHR), 0, stream, p);
}
```

```cpp
#include <hip/hip_runtime.h>
#include <stdint.h>

typedef unsigned u32;
typedef unsigned long long u64;
typedef unsigned short bf16_t;
typedef u32 u32x2 __attribute__((ext_vector_type(2)));
typedef u32 u32x4 __attribute__((ext_vector_type(4)));
typedef float f32x2 __attribute__((ext_vector_type(2)));
typedef float f32x4 __attribute__((ext_vector_type(4)));
typedef short s16x4 __attribute__((ext_vector_type(4)));
typedef __bf16 bf16x2 __attribute__((ext_vector_type(2)));
typedef __bf16 bf16x8 __attribute__((ext_vector_type(8)));
typedef __amdgpu_buffer_rsrc_t rsrc_t;
#define GAS __attribute__((address_space(1)))
#define LAS __attribute__((address_space(3)))

constexpr int CODE_LINES = 95;
constexpr int ANCH = 55;
constexpr int PT0 = 0, PTAU = 235, LT0 = 82;
constexpr int LW = 5;
constexpr int TT0 = 388, TTAU = 12;
constexpr int QTIME = 272, QD = 90;
constexpr int FTIME = 365;
constexpr int T2ABS = 533, DLY2 = 10;
constexpr int T3ABS = 615;
constexpr int GSL = 35;

constexpr int D = 512, NQ = 4096, SK = 65536, WIN = 4096, DH = 128, F = 1792;
constexpr int NWG = 256, NTHR = 512;
constexpr int AUX_NT = 2, AUX_SC1 = 16;
constexpr u32 OOB = 0x80000000u;
constexpr u32 WS_EPOCH = 0;
constexpr u32 WS_TS = 4096;
constexpr u32 WS_ML = 32768;
constexpr u32 WS_Q = 131072;
constexpr u32 WS_F = WS_Q + 131072;
constexpr u32 WS_A = 1u << 20;
constexpr u32 MSG = 64, AROW = 32 * MSG;
constexpr int NCONS = 32;
constexpr u32 WS_END = 2u << 20;
constexpr u32 SPIN_CAP = 200000, LSPIN_CAP = 8000000;
constexpr int L_W = 0  , L_K = 32768, L_V = 65536, L_T = 98304, L_X = 122880, L_Q = L_X + 1024,
              L_P = L_Q + 1024, L_M = L_P + 1280, L_OP = L_M + 64, L_FLAG = L_OP + 64,
              L_XN = L_FLAG + 64, L_EP = L_XN + 512, L_DUMP = L_EP + 256, L_END = L_DUMP + 256;
constexpr int FL_PROG = 0  , FL_X = 2, FL_T = 3, FL_S = 4  , FL_O = 8  , FL_VI = 10  , FL_W = 12  ;
constexpr int G_F = 8, G_K = 16, G_V = 24;

__device__ __forceinline__ float bflo(u32 u) { return __uint_as_float(u << 16); }
__device__ __forceinline__ float bfhi(u32 u) { return __uint_as_float(u & 0xffff0000u); }
__device__ __forceinline__ float asf(u32 u) { return __uint_as_float(u); }
__device__ __forceinline__ u32 asu(float f) { return __float_as_uint(f); }
__device__ __forceinline__ u32 pk_bf16(float lo, float hi) {
  return __builtin_bit_cast(u32, __builtin_convertvector((f32x2{lo, hi}), bf16x2));
}
__device__ __forceinline__ float rbf(float x) { return (float)(__bf16)x; }
__device__ __forceinline__ float vmax(float a, float b) { return __builtin_elementwise_maximum(a, b); }
__device__ __forceinline__ float vmax3(float a, float b, float c) { return vmax(vmax(a, b), c); }
__device__ __forceinline__ float exp_sub(float a, float mx) { return __builtin_amdgcn_exp2f((a - mx) * 1.4426950408889634f); }
__device__ __forceinline__ float dot2(u32 a, u32 b, float acc) {
  return __builtin_amdgcn_fdot2_f32_bf16(__builtin_bit_cast(bf16x2, a), __builtin_bit_cast(bf16x2, b), acc, false);
}
__device__ __forceinline__ u32 perm_lo(u32 a, u32 b) { return __builtin_amdgcn_perm(b, a, 0x05040100u); }
__device__ __forceinline__ u32 perm_hi(u32 a, u32 b) { return __builtin_amdgcn_perm(b, a, 0x07060302u); }
template <int CTRL> __device__ __forceinline__ float dpp(float x) {
  return __builtin_bit_cast(float, __builtin_amdgcn_mov_dpp(__builtin_bit_cast(int, x), CTRL, 0xf, 0xf, true));
}
constexpr int XOR1 = 0xB1, XOR2 = 0x4E, XOR7 = 0x141, XOR8 = 0x128, ROR4 = 0x124;
__device__ __forceinline__ float swap32_add(float a, float b) {
  auto s = __builtin_amdgcn_permlane32_swap(asu(a), asu(b), false, false);
  return asf(s[0]) + asf(s[1]);
}
__device__ __forceinline__ float swap16_add(float a, float b) {
  auto s = __builtin_amdgcn_permlane16_swap(asu(a), asu(b), false, false);
  return asf(s[0]) + asf(s[1]);
}
__device__ __forceinline__ float swap32_max(float a) {
  auto s = __builtin_amdgcn_permlane32_swap(asu(a), asu(a), false, false);
  return vmax(asf(s[0]), asf(s[1]));
}
__device__ __forceinline__ float swap16_max(float a) {
  auto s = __builtin_amdgcn_permlane16_swap(asu(a), asu(a), false, false);
  return vmax(asf(s[0]), asf(s[1]));
}
__device__ __forceinline__ u32 tag_of(u32 e) { return (e & 0x3fffu) | ((e << 1) & 0x3fff8000u) | ((e << 2) & 0x80000000u); }
__device__ __forceinline__ u32 qtag_of(u32 e) { return e * 0x9E3779B1u; }
constexpr u32 TAG_HI = 0x40000000u;
__device__ __forceinline__ rsrc_t mk_rsrc(const void* p, u32 bytes) { return __builtin_amdgcn_make_buffer_rsrc((void*)p, 0, bytes, 0x00020000); }
#define LD16(rs, voff, soff) __builtin_amdgcn_raw_buffer_load_b128((rs), (voff), (soff), AUX_SC1)
#define LD8(rs, voff, soff) __builtin_amdgcn_raw_buffer_load_b64((rs), (voff), (soff), AUX_SC1)
#define ST16(v, rs, voff, soff) __builtin_amdgcn_raw_buffer_store_b128((v), (rs), (voff), (soff), AUX_SC1)
#define ST8(v, rs, voff, soff) __builtin_amdgcn_raw_buffer_store_b64((v), (rs), (voff), (soff), AUX_SC1)
#define ST4(v, rs, voff, soff) __builtin_amdgcn_raw_buffer_store_b32((v), (rs), (voff), (soff), AUX_SC1)
#define DMA16(rs, ldsoff, voff, soff, imm) __builtin_amdgcn_raw_ptr_buffer_load_lds((rs), (LAS void*)(lds + (ldsoff)), 16, (voff), (soff), (imm), AUX_NT)
#define KEEP(v) asm volatile("" :: "v"(v))
#define PIN() __builtin_amdgcn_sched_barrier(0)
#define OPAQUE(v) asm volatile("" : "+v"(v))
#define LDSA(T, a) ((LAS T*)(uintptr_t)(a))
#define NOSPLIT(bytes) asm volatile("s_branch 1f\n\t.p2align 12,," #bytes "\n1:" ::: "memory")
#define MEMBAR() asm volatile("" ::: "memory")
#define LDSP(T, off) ((LAS T*)(lds + (off)))
template <int N> __device__ __forceinline__ void wait_vm() { asm volatile("s_waitcnt vmcnt(%0)" :: "n"(N) : "memory"); }
__device__ __forceinline__ u32 clk() { return (u32)__builtin_amdgcn_s_memrealtime(); }
__device__ __forceinline__ void wait_until(u32 t0, int dt) {
  for (;;) {
    const int left = dt - (int)(clk() - t0);
    if (left <= 0) break;
    if (left > 24) __builtin_amdgcn_s_sleep(4);
  }
  MEMBAR();
}
__device__ __forceinline__ void lds_signal(LAS unsigned char* lds, int word, u32 v) {
  MEMBAR();
  *(volatile LAS u32*)(lds + L_FLAG + 4 * word) = v;
}
__device__ __forceinline__ u32 peek1(LAS unsigned char* lds, int word) { const u32 v = *(volatile LAS u32*)(lds + L_FLAG + 4 * word); MEMBAR(); return v; }
__device__ __forceinline__ u32x2 peek2(LAS unsigned char* lds, int word) { const u32x2 v = *(volatile LAS u32x2*)(lds + L_FLAG + 4 * word); MEMBAR(); return v; }
__device__ __forceinline__ u32x4 peek4(LAS unsigned char* lds, int word) { const u32x4 v = *(volatile LAS u32x4*)(lds + L_FLAG + 4 * word); MEMBAR(); return v; }
__device__ __forceinline__ bool ge(u32 v, u32 need) { return (u32)__builtin_amdgcn_readfirstlane(v) >= need; }
__device__ __forceinline__ bool ge(u32x2 v, u32 need) { return ge(min(v[0], v[1]), need); }
__device__ __forceinline__ bool ge(u32x4 v, u32 need) { return ge(min(min(v[0], v[1]), min(v[2], v[3])), need); }
__device__ __forceinline__ u32x4 tr_frag(u32 a0, u32 a1) {
  const u32x2 lo = __builtin_bit_cast(u32x2, __builtin_amdgcn_ds_read_tr16_b64_v4i16(LDSA(s16x4, a0)));
  const u32x2 hi = __builtin_bit_cast(u32x2, __builtin_amdgcn_ds_read_tr16_b64_v4i16(LDSA(s16x4, a1)));
  return u32x4{lo[0], lo[1], hi[0], hi[1]};
}
__device__ __forceinline__ u32 neq2(u32 a, u32 b, u32 t) { return __builtin_amdgcn_bitop3_b32(a, b, t, 0x7e); }
__device__ __forceinline__ bool all_zero(u32 bad) { OPAQUE(bad); return __builtin_amdgcn_ballot_w64(bad != 0) == 0; }
__device__ __forceinline__ f32x4 mfma(u32x4 a, u32x4 b, f32x4 c) {
  return __builtin_amdgcn_mfma_f32_16x16x32_bf16(__builtin_bit_cast(bf16x8, a), __builtin_bit_cast(bf16x8, b), c, 0, 0, 0);
}
__device__ __forceinline__ void lds_wait1(LAS unsigned char* lds, int word, u32 need) {
  for (u32 n = 0; *(volatile LAS u32*)(lds + L_FLAG + 4 * word) < need && n < LSPIN_CAP; ++n) { }
  MEMBAR();
}
__device__ __forceinline__ void lds_wait4(LAS unsigned char* lds, int word, u32 need) {
  for (u32 n = 0; n < LSPIN_CAP; ++n) {
    const u32x4 v = *(volatile LAS u32x4*)(lds + L_FLAG + 4 * word);
    if (min(min(v[0], v[1]), min(v[2], v[3])) >= need) break;
  }
  MEMBAR();
}

__device__ __forceinline__ void lds_wait_prog(LAS unsigned char* lds, u32 need) {
  for (u32 n = 0; n < LSPIN_CAP; ++n) {
    const u32x2 v = *(volatile LAS u32x2*)(lds + L_FLAG + 4 * FL_PROG);
    if (min(v[0], v[1]) >= need) break;
  }
  MEMBAR();
}
__device__ __forceinline__ void pace(u32 flag_addr, u32& fl, u32 n, u32 deadline, u32 win) {
  u32 k, x;
  asm volatile(
      "1:\n\t"
      "s_memrealtime vcc\n\t"
      "s_getreg_b32 %[x], hwreg(HW_REG_IB_STS)\n\t"
      "s_and_b32 %[k], %[x], 15\n\t"
      "s_bfe_u32 %[x], %[x], 0x20016\n\t"
      "s_lshl4_add_u32 %[k], %[x], %[k]\n\t"
      "s_add_u32 %[k], %[k], 3\n\t"
      "s_lshr_b32 %[k], %[k], 2\n\t"
      "s_cmp_ge_u32 %[k], %[fl]\n\t"
      "s_cbranch_scc1 3f\n\t"
      "s_waitcnt vmcnt(28)\n\t"
      "s_cmp_gt_u32 %[k], 6\n\t"
      "s_cbranch_scc1 2f\n\t"
      "s_waitcnt vmcnt(24)\n\t"
      "s_cmp_gt_u32 %[k], 5\n\t"
      "s_cbranch_scc1 2f\n\t"
      "s_waitcnt vmcnt(20)\n\t"
      "s_cmp_gt_u32 %[k], 4\n\t"
      "s_cbranch_scc1 2f\n\t"
      "s_waitcnt vmcnt(16)\n\t"
      "s_cmp_gt_u32 %[k], 3\n\t"
      "s_cbranch_scc1 2f\n\t"
      "s_waitcnt vmcnt(12)\n\t"
      "s_cmp_gt_u32 %[k], 2\n\t"
      "s_cbranch_scc1 2f\n\t"
      "s_waitcnt vmcnt(8)\n\t"
      "s_cmp_gt_u32 %[k], 1\n\t"
      "s_cbranch_scc1 2f\n\t"
      "s_waitcnt vmcnt(4)\n\t"
      "s_cmp_gt_u32 %[k], 0\n\t"
      "s_cbranch_scc1 2f\n\t"
      "s_waitcnt vmcnt(0)\n\t"
      "2:\n\t"
      "s_mov_b32 %[fl], %[k]\n\t"
      "s_lshl_b32 %[x], %[k], 1\n\t"
      "s_sub_u32 %[x], %[n], %[x]\n\t"
      "v_mov_b32 v31, %[x]\n\t"
      "ds_write_b32 %[a], v31\n\t"
      "3:\n\t"
      "s_waitcnt lgkmcnt(0)\n\t"
      "s_sub_u32 %[x], vcc_lo, %[dl]\n\t"
      "s_cmp_lt_i32 %[x], 0\n\t"
      "s_cbranch_scc1 1b\n\t"
      "s_cmp_ge_u32 %[fl], %[win]\n\t"
      "s_cbranch_scc1 1b\n\t"
      : [k] "=&s"(k), [x] "=&s"(x), [fl] "+s"(fl)
      : [n] "s"(n), [dl] "s"(deadline), [win] "s"(win), [a] "v"(flag_addr)
      : "scc", "vcc", "v31", "memory");
}


__device__ __forceinline__ int relaxed(int org, int slack) {
  int last = max(org, __builtin_amdgcn_mov_dpp(org, XOR1, 0xf, 0xf, true));
  last = max(last, __builtin_amdgcn_mov_dpp(last, XOR2, 0xf, 0xf, true));
  last = max(last, __builtin_amdgcn_mov_dpp(last, XOR7, 0xf, 0xf, true));
  return min(last, org + slack);
}

struct Params {
  const bf16_t* x; const bf16_t* kc; const bf16_t* vc; const bf16_t* wq; const bf16_t* wo;
  const bf16_t* w1; const bf16_t* w3; const bf16_t* w2; bf16_t* out; unsigned char* ws;
};
#define SETFLAG(w, v) (*(volatile LAS u32*)(lds + L_FLAG + 4 * (w)) = (v))

typedef u32 u32x16 __attribute__((ext_vector_type(16)));
template <typename T> __device__ __forceinline__ T* mk_ptr(u32 lo, u32 hi) { return (T*)(((u64)hi << 32) | lo); }

__global__ void __launch_bounds__(NTHR, 2) __attribute__((aligned(4096))) fused_decode(Params pk) {
  __shared__ __attribute__((aligned(1024))) unsigned char smem[L_END];
  LAS unsigned char* lds = (LAS unsigned char*)smem;
  u32x16 ka; u32x4 kb;
  asm volatile("s_load_dwordx16 %0, %2, 0x0\n\ts_load_dwordx4 %1, %2, 0x40" : "=&s"(ka), "=&s"(kb) : "s"(__builtin_amdgcn_kernarg_segment_ptr()));
  const int tid = threadIdx.x, lane = tid & 63, wave = __builtin_amdgcn_readfirstlane(tid >> 6);
  const int b = blockIdx.x, g = b & 7, j = b >> 3;
  const u64 pc = __builtin_amdgcn_s_getpc() & ~(u64)4095;
  if (tid >= 128 && tid < 132 && j * 4 + lane < CODE_LINES) {
    __builtin_amdgcn_global_load_lds((const GAS void*)(pc + (u64)((j * 4 + lane) * 128)), (LAS void*)(lds + L_DUMP), 4, 0, 0);
  }
  if (tid < 16) *LDSP(u32, L_FLAG + 4 * tid) = 0u;
  const int kap = j & 3;
  const u32 wrow = 8 * (lane >> 5) + ((lane >> 3) & 3), wcol = ((lane & 7) ^ ((lane >> 4) << 1)) * 16;
  u32 qvo = wrow * (NQ * 2) + wcol, xvo = lane * 16;
  u32 qs0 = (u32)(128 * kap + 32 * (wave - 3)) * (NQ * 2) + (g * 512 + (j >> 2) * 64) * 2;
  u32 qs1 = qs0 + 16 * NQ * 2 - 1024, qs2 = qs0 + 4 * NQ * 2 - 2048, qs3 = qs0 + 20 * NQ * 2 - 3072;
  OPAQUE(qvo); OPAQUE(xvo);
  asm volatile("s_waitcnt lgkmcnt(0)" : "+s"(ka), "+s"(kb), "+s"(qs0), "+s"(qs1), "+s"(qs2), "+s"(qs3) :: "memory");
  Params p;
  p.x = mk_ptr<const bf16_t>(ka[0], ka[1]); p.kc = mk_ptr<const bf16_t>(ka[2], ka[3]); p.vc = mk_ptr<const bf16_t>(ka[4], ka[5]);
  p.wq = mk_ptr<const bf16_t>(ka[6], ka[7]); p.wo = mk_ptr<const bf16_t>(ka[8], ka[9]); p.w1 = mk_ptr<const bf16_t>(ka[10], ka[11]);
  p.w3 = mk_ptr<const bf16_t>(ka[12], ka[13]); p.w2 = mk_ptr<const bf16_t>(ka[14], ka[15]);
  p.out = mk_ptr<bf16_t>(kb[0], kb[1]); p.ws = mk_ptr<unsigned char>(kb[2], kb[3]);
  if (wave == 1) {
    __builtin_amdgcn_raw_ptr_buffer_load_lds(mk_rsrc(p.x, D * 2), (LAS void*)(lds + L_X), 16, xvo, 0, 0, 0);
    __builtin_amdgcn_raw_ptr_buffer_load_lds(mk_rsrc(p.ws, 4), (LAS void*)(lds + L_EP), 4, 0, WS_EPOCH, 0, AUX_SC1);
  }
  __builtin_amdgcn_s_barrier();
  MEMBAR();
  if (wave >= 3 && wave < 7) {
    const rsrc_t rw = mk_rsrc(p.wq, D * NQ * 2);
    const int lo = L_W + (wave - 3) * 4096;
    DMA16(rw, lo, qvo, qs0, 0); DMA16(rw, lo, qvo, qs1, 1024); DMA16(rw, lo, qvo, qs2, 2048); DMA16(rw, lo, qvo, qs3, 3072);
  }
  __builtin_amdgcn_s_barrier();
  MEMBAR();
  const u32 tstart = clk() - ANCH;
  if (wave == 2 && j >= 24 && j < 30) {
    const int k = j - 24;
    const bf16_t* ptr = k == 0 ? p.w1 : k == 1 ? p.w3 : k == 2 ? p.kc : k == 3 ? p.vc : k == 4 ? p.w2 : p.wo;
    const u32 first = (k | 1) == 3 ? (u32)(g * SK + (SK - WIN)) * (DH * 2) : k == 5 ? (u32)g * 512 * D * 2 : (u32)g * 128u;
    const u32 step = (k | 1) == 3 ? 149760u : k == 5 ? 74880u : 261952u;
    __builtin_amdgcn_raw_ptr_buffer_load_lds(mk_rsrc(ptr, first + 7 * step + 4), (LAS void*)(lds + L_DUMP), 4, lane < 8 ? lane * step : OOB, first, 0, AUX_NT);
  }
  const int fline = g + 8 * (j >> 3), fmem = j & 7, fchunk = fline * 8 + fmem;
  const bool has_ffn = fline < 28;

  if (wave == 1 || wave == 2) {
    const int cw = wave - 1, gam = lane >> 4;
    const rsrc_t rp = mk_rsrc(p.ws, (cw == 0 || has_ffn) ? WS_END : 0u);
    const u32 pub_off = cw == 0 ? WS_Q + ((g * 4 + kap) * 512 + (j >> 2) * 64) * 4 : WS_F + (fline * 8 + fmem) * 512;
    const int tr8 = 4 * (gam & 1) + ((lane >> 2) & 3);
    const u32 wb = (u32)(uintptr_t)lds + L_W + cw * 16384 + (gam >> 1) * 1024 + tr8 * 128 + (lane & 3) * 8;
    PIN();
    __builtin_amdgcn_s_waitcnt(0x0F70);
    MEMBAR();
    if (cw == 0) SETFLAG(FL_X, 1u); else lds_wait1(lds, FL_X, 1u);
    PIN();
    const u32x4 xv = *LDSP(u32x4, L_X + lane * 16);
    const u32 xd = *LDSP(u32, L_X + kap * 256 + lane * 4), x0 = *LDSP(u32, L_X);
    const u32 cnt = *LDSP(u32, L_EP) + 1u;
    u32 tag = tag_of(cnt) | TAG_HI, gp = lane * 8;
    u32 tq = qtag_of(cnt), gp4 = lane < 16 ? (u32)lane * 16 : OOB;
    OPAQUE(tag); OPAQUE(gp); OPAQUE(tq); OPAQUE(gp4);
    const float sh = bflo(x0);
    float s1 = 0.f, s2 = 0.f;
#pragma unroll
    for (int k = 0; k < 4; ++k) {
      const float d0 = bflo(xv[k]) - sh, d1 = bfhi(xv[k]) - sh;
      s1 += d0 + d1; s2 = fmaf(d0, d0, fmaf(d1, d1, s2));
    }
    float ss = swap32_add(s1, s2);
    ss = swap16_add(ss, ss); ss += dpp<XOR8>(ss); ss += dpp<XOR7>(ss); ss += dpp<XOR2>(ss); ss += dpp<XOR1>(ss);
    auto sx = __builtin_amdgcn_permlane32_swap(asu(ss), asu(ss), false, false);
    const float md = asf(sx[0]) * (1.f / D), var = fmaxf(fmaf(-md, md, asf(sx[1]) * (1.f / D)), 0.f);
    const float mb = rbf(sh + md), isd = __builtin_amdgcn_rcpf(rbf(__builtin_amdgcn_sqrtf(rbf(rbf(var) + 1e-5f))));
    const u32 dd = pk_bf16(bflo(xd) - mb, bfhi(xd) - mb);
    *LDSP(u32, L_XN + cw * 256 + lane * 4) = pk_bf16(bflo(dd) * isd, bfhi(dd) * isd);
    PIN();
    u32x4 xa[4];
    u32 ad[4];
#pragma unroll
    for (int k = 0; k < 4; ++k) { xa[k] = *LDSP(u32x4, L_XN + cw * 256 + k * 64 + gam * 16); ad[k] = wb + ((k ^ (tr8 >> 1)) << 5); OPAQUE(ad[k]); }
    if (cw) lds_wait_prog(lds, G_F); else lds_wait4(lds, FL_W, 1u);
    f32x4 acc[4];
#pragma unroll
    for (int t = 0; t < 4; ++t) acc[t] = f32x4{0.f, 0.f, 0.f, 0.f};
    u32x4 wf[16];
#pragma unroll
    for (int i = 0; i < 16; ++i) wf[i] = tr_frag(ad[i & 3] + (i >> 2) * 4096, ad[i & 3] + (i >> 2) * 4096 + 2048);
    PIN();
#pragma unroll
    for (int i = 0; i < 16; ++i) acc[i & 3] = mfma(xa[i >> 2], wf[i], acc[i & 3]);
    const float t01 = (gam & 1) ? acc[1][0] : acc[0][0], t23 = (gam & 1) ? acc[3][0] : acc[2][0], t = (gam & 2) ? t23 : t01;
    if (cw == 0) {
      u32x4 w;
#pragma unroll
      for (int k = 0; k < 4; ++k) w[k] = __builtin_amdgcn_perm(asu(acc[k][0]) + 0x80u, tq, 0x07060500u + k);
      ST16(w, rp, gp4, pub_off);
    } else ST8((u32x2{asu(t), tag}), rp, gp, pub_off);
    if (cw == 0 && j == 0) ST8((u32x2{tstart, tag}), rp, lane < 16 ? lane * 8 : OOB, WS_TS + g * 128);
    PIN();
  }

  if (wave == 0 || wave == 7) {
    const int ld = wave & 1;
    const u32 pw = (u32)(uintptr_t)(lds + L_FLAG + 4 * (FL_PROG + ld));
    u32 n = 4 + ld, fl = 0;
    const rsrc_t rw = mk_rsrc((fmem & 4) ? p.w3 : p.w1, has_ffn ? D * F * 2 : 0);
    u32 fso = (u32)(128 * kap + 32 * ld) * (F * 2) + fline * 128;
    const u32 fvo = wrow * (F * 2) + wcol;
    const int rr = lane >> 4, cc = lane & 15;
    rsrc_t rk = mk_rsrc(p.kc, 8u * SK * DH * 2);
    u32 v0 = rr * 256 + ((cc ^ rr) << 4), v1 = rr * 256 + ((cc ^ (4 + rr)) << 4), v2 = rr * 256 + ((cc ^ (8 + rr)) << 4), v3 = rr * 256 + ((cc ^ (12 + rr)) << 4);
    const u32 kvo = (u32)(g * SK + (SK - WIN)) * (DH * 2);
    u32 lo = L_K + ld * 4096, ti = ld;
    PIN();
#pragma unroll 1
    for (; n < G_V; n += 2) {
      pace(pw, fl, n, tstart + max((int)LT0, (int)(PT0 + ((n * PTAU) >> 4))), LW); ++fl;
      if ((n >> 2) == G_F / 4 - 1) {
        const int fo = L_W + 16384 + (n & 3) * 4096;
        DMA16(rw, fo, fvo, fso, 0); DMA16(rw, fo, fvo, fso + 16 * F * 2 - 1024, 1024);
        DMA16(rw, fo, fvo, fso + 4 * F * 2 - 2048, 2048); DMA16(rw, fo, fvo, fso + 20 * F * 2 - 3072, 3072);
        fso += 64 * F * 2;
      } else {
        if ((n >> 1) == 8) {
          rk = mk_rsrc(p.vc, 8u * SK * DH * 2);
          v0 = v1 = rr * 256 + ((cc ^ (rr << 1)) << 4); v2 = v3 = rr * 256 + ((cc ^ ((rr | 4) << 1)) << 4);
        }
        const u32 so = kvo + ((ti & 7) << 17) + (((j + 9 * (ti & 7)) & 31) << 12);
        DMA16(rk, lo, v0, so, 0); DMA16(rk, lo, v1, so, 1024); DMA16(rk, lo, v2, so, 2048); DMA16(rk, lo, v3, so, 3072);
        lo += 8192; ti += 2;
      }
    }
    SETFLAG(FL_VI + ld, 1u);
    NOSPLIT(300);
    PIN();
    pace(pw, fl, n, tstart, 4);
    wait_vm<8>(); SETFLAG(FL_PROG + ld, max(n - 4, n - 2 * fl));
    wait_vm<4>(); SETFLAG(FL_PROG + ld, max(n - 2, n - 2 * fl));
    wait_vm<0>(); SETFLAG(FL_PROG + ld, n);
    PIN();
    if (!ld || b >= NCONS) return;
  }

  __builtin_amdgcn_s_waitcnt(0x0F70);
  MEMBAR();
  if (wave >= 3 && wave < 7) SETFLAG(FL_W - 3 + wave, 1u);
  lds_wait1(lds, FL_X, 1u);
  const u32 count = __builtin_amdgcn_readfirstlane(*LDSP(u32, L_EP)) + 1u;
  const u32 tag2 = tag_of(count), tag = tag2 | TAG_HI;
  const rsrc_t rs = mk_rsrc(p.ws, WS_END);

  if (wave == 7) {
    const int r = lane >> 4, c = lane & 15;
    u32x2 tsv;
    u32 spins = 0;
    do { MEMBAR(); tsv = LD8(rs, (lane & 7) * 128, WS_TS); } while (!all_zero(tsv[1] ^ tag) && ++spins < SPIN_CAP);
    const u32 due = tstart + relaxed((int)(tsv[0] - tstart), GSL) + T3ABS;
    const u32 one = ((c >> 2) & 1) ? 0x3f800000u : 0x00003f80u;
    u32x4 sel = {c < 8 ? one : 0u, 0u, c < 8 ? 0u : one, 0u};
    u32 xx = *LDSP(u32, L_X + b * 32 + (c & 3) * 8 + (r >> 1) * 4), gl = lane * 16, go2 = lane < 4 ? (u32)lane * 8 : OOB;
    OPAQUE(sel); OPAQUE(xx); OPAQUE(gl); OPAQUE(go2);
    const rsrc_t rout = mk_rsrc(p.out, D * 2);
    ST8((u32x2{0u, 0u}), rout, go2, b * 32);
    bool bump = b == 0;
    if (bump) {
      wait_until(tstart, T2ABS - 20);
      const u32x2 mv = LD8(rs, (lane & 7) * 4096, WS_ML);
      if (all_zero(mv[1] ^ tag)) { ST4(count, rs, lane == 0 ? 0u : OOB, WS_EPOCH); bump = false; }
    }
    u32x4 ov[16];
    u32 left = 0xffu;
    NOSPLIT(1340);
    while (left) {
      const u32 now = clk();
      const u32 go = left & (u32)__builtin_amdgcn_ballot_w64((int)(now - due) >= 0 || (int)(now - tstart) > 4000);
      if (!go) continue;
#pragma unroll
      for (int q = 0; q < 8; ++q)
        if (__builtin_expect((go & (1u << q)) != 0, 1)) { ov[2 * q] = LD16(rs, gl, WS_A + (q * 32 + b) * AROW); ov[2 * q + 1] = LD16(rs, gl + 1024, WS_A + (q * 32 + b) * AROW); }
      left &= ~go;
    }
    PIN();
    float s;
    spins = 0;
    for (;;) {
      f32x4 d0 = {0.f, 0.f, 0.f, 0.f}, d1 = {0.f, 0.f, 0.f, 0.f};
      u32 bad = 0;
#pragma unroll
      for (int t = 0; t < 16; t += 2) {
        d0 = mfma(sel, ov[t], d0); d1 = mfma(sel, ov[t + 1], d1);
        bad |= neq2(ov[t][1], ov[t][3], tag2) | neq2(ov[t + 1][1], ov[t + 1][3], tag2);
      }
      s = d0[0] + d1[0];
      if (__builtin_expect(all_zero(bad), 1) || ++spins > SPIN_CAP) break;
      MEMBAR();
#pragma unroll
      for (int q = 0; q < 8; ++q)
        if (!all_zero(neq2(ov[2 * q][1], ov[2 * q][3], tag2) | neq2(ov[2 * q + 1][1], ov[2 * q + 1][3], tag2))) {
          ov[2 * q] = LD16(rs, gl, WS_A + (q * 32 + b) * AROW); ov[2 * q + 1] = LD16(rs, gl + 1024, WS_A + (q * 32 + b) * AROW);
        }
    }
    PIN();
    s += dpp<XOR8>(s); s += dpp<ROR4>(s);
    auto pr = __builtin_amdgcn_permlane16_swap(asu(s), asu(s), false, false);
    const u32 res = pk_bf16(bflo(xx) + asf(pr[0]), bfhi(xx) + asf(pr[1]));
    auto rr = __builtin_amdgcn_permlane32_swap(res, res, false, false);
    ST8((u32x2{rr[0], rr[1]}), rout, go2, b * 32);
    if (bump) ST4(count, rs, lane == 0 ? 0u : OOB, WS_EPOCH);
    PIN();
    return;
  }

  if (wave == 2) {
    const int r8 = lane >> 3, c8 = lane & 7;
    const rsrc_t rf = mk_rsrc(p.ws, has_ffn ? WS_END : 0u);
    NOSPLIT(1700);
    wait_until(tstart, FTIME);
    const u32x2 tv = LD8(rs, (lane & 7) * 128, WS_TS);
    u32x2 fv = LD8(rf, r8 * 512 + c8 * 8, WS_F + (fchunk >> 3) * 4096 + (fchunk & 7) * 64);
    {
      const int hc = (lane >> 2) & 3;
      lds_wait4(lds, FL_S, 2u);
      const f32x4 mm = *LDSP(f32x4, L_M + hc * 16), z = {0.f, 0.f, 0.f, 0.f};
      const u32x4 ones = {0x3f803f80u, 0x3f803f80u, 0x3f803f80u, 0x3f803f80u};
      const float M = vmax(vmax3(mm[0], mm[1], mm[2]), mm[3]);
      float L = 0.f;
#pragma unroll
      for (int k = 0; k < 4; ++k) L = fmaf(exp_sub(mm[k], M), mfma(ones, *LDSP(u32x4, L_P + hc * 320 + k * 64 + (lane >> 4) * 16), z)[0], L);
      ST8((u32x2{asu((lane & 1) ? L : M), tag}), rs, lane < 16 ? (u32)lane * 8 : OOB, WS_ML + (g * 32 + j) * 128);
    }
    lds_wait4(lds, FL_S, 3u);
    const u32 ta = clk();
    PIN();
    const int org = all_zero(tv[1] ^ tag) ? __builtin_amdgcn_readlane(relaxed((int)(tv[0] - tstart), GSL), g) : 0;
    const int t2 = max(min(org, 200) + (int)T2ABS, (int)(ta - tstart) + DLY2);
    u32 gm = r8 * 128, gv = r8 * AROW + c8 * 8, aow = (u32)(uintptr_t)lds + (lane < 8 ? L_OP : L_DUMP) + lane * 4;
    OPAQUE(gm); OPAQUE(gv); OPAQUE(aow);
    float a0, a1;
    u32 spins = 0;
    u32x4 ml[4];
    u32x2 av[4];
    wait_until(tstart, t2 - 15);
#pragma unroll
    for (int t = 0; t < 4; ++t) ml[t] = LD16(rs, gm + t * 1024, WS_ML + g * 4096 + (j >> 3) * 32);
    wait_until(tstart, t2);
#pragma unroll
    for (int t = 0; t < 4; ++t) av[t] = LD8(rs, gv, WS_A + (g * 32 + 8 * t) * AROW + j * MSG);
    PIN();
    {
      u32 spins = 0;
      if (__builtin_expect(has_ffn && !all_zero(fv[1] ^ tag), 0)) do {
        MEMBAR();
        fv = LD8(rf, r8 * 512 + c8 * 8, WS_F + (fchunk >> 3) * 4096 + (fchunk & 7) * 64);
      } while (!all_zero(fv[1] ^ tag) && ++spins <= SPIN_CAP);
      float s = asf(fv[0]);
      s += dpp<XOR8>(s); s = swap16_add(s, s);
      s = rbf(s);
      auto sw = __builtin_amdgcn_permlane32_swap(asu(s), asu(s), false, false);
      const float ga = asf(sw[0]), up = asf(sw[1]);
      const float hv = rbf(ga * __builtin_amdgcn_rcpf(1.f + __expf(-ga))) * up;
      const u32 hp = pk_bf16(hv, dpp<XOR1>(hv));
      if (lane < 8 && !(lane & 1)) *LDSP(u32, L_OP + 32 + 2 * lane) = hp;
      lds_signal(lds, FL_O + 1, 1u);
      PIN();
    }
    auto combine = [&](const u32x4 (&m4)[4], const u32x2 (&v2)[4]) __attribute__((always_inline)) -> bool {
      PIN();
      float M = vmax(vmax3(asf(m4[0][0]), asf(m4[1][0]), asf(m4[2][0])), asf(m4[3][0]));
      M = vmax(M, dpp<XOR8>(M)); M = swap16_max(M); M = swap32_max(M);
      float wt[4], L = 0.f;
      u32 bad = 0;
#pragma unroll
      for (int t = 0; t < 4; ++t) {
        wt[t] = exp_sub(asf(m4[t][0]), M); L = fmaf(wt[t], asf(m4[t][2]), L);
        bad |= neq2(m4[t][1], m4[t][3], tag);
      }
      L += dpp<XOR8>(L); L = swap16_add(L, L); L = swap32_add(L, L);
      const float inv = __builtin_amdgcn_rcpf(L);
#pragma unroll
      for (int t = 0; t < 4; ++t) { wt[t] *= inv; KEEP(wt[t]); }
      KEEP(bad);
      PIN();
      a0 = fmaf(wt[0], bflo(v2[0][0]), wt[1] * bflo(v2[1][0])) + fmaf(wt[2], bflo(v2[2][0]), wt[3] * bflo(v2[3][0]));
      a1 = fmaf(wt[0], bfhi(v2[0][0]), wt[1] * bfhi(v2[1][0])) + fmaf(wt[2], bfhi(v2[2][0]), wt[3] * bfhi(v2[3][0]));
      return all_zero(bad | neq2(v2[0][1], v2[1][1], tag) | neq2(v2[2][1], v2[3][1], tag));
    };
    if (__builtin_expect(!combine(ml, av), 0)) {
#pragma unroll 1
      for (bool ok = false; !ok && ++spins <= SPIN_CAP;) {
        u32x4 mr[4];
        u32x2 ar[4];
        __builtin_amdgcn_s_sleep(4);
        MEMBAR();
#pragma unroll
        for (int t = 0; t < 4; ++t) mr[t] = LD16(rs, gm + t * 1024, WS_ML + g * 4096 + (j >> 3) * 32);
#pragma unroll
        for (int t = 0; t < 4; ++t) ar[t] = LD8(rs, gv, WS_A + (g * 32 + 8 * t) * AROW + j * MSG);
        ok = combine(mr, ar);
      }
    }
    PIN();
    float s = swap32_add(a0, a1);
    s = swap16_add(s, s); s += dpp<XOR8>(s);
    auto sw = __builtin_amdgcn_permlane32_swap(asu(s), asu(s), false, false);
    *LDSA(u32, aow) = pk_bf16(asf(sw[0]), asf(sw[1]));
    lds_signal(lds, FL_O, 1u);
    PIN();
    return;
  }

  if (wave == 1) {
    const rsrc_t ro = mk_rsrc(p.wo, NQ * D * 2), r2 = mk_rsrc(p.w2, has_ffn ? F * D * 2 : 0);
    const u32 so = (u32)(g * 512 + (j >> 1) * 32 + (j & 1) * 8) * (D * 2), s2 = (u32)(fchunk * 8) * (D * 2);
    NOSPLIT(600);
    wait_until(tstart, TT0 - 12);
    for (u32 it = 0; it < LSPIN_CAP && !ge(peek2(lds, FL_VI), 1u); ++it) { }
    wait_until(tstart, TT0 + (j >> 3) * TTAU);
#pragma unroll 1
    for (int m = 0; m < 4; ++m) {
      DMA16(ro, L_T + 8192 + m * 4096, lane * 16, so + m * 2048, 0); DMA16(ro, L_T + 8192 + m * 4096, lane * 16, so + m * 2048, 1024);
      DMA16(ro, L_T + 8192 + m * 4096, lane * 16, so + m * 2048 + 16384 - 2048, 2048); DMA16(ro, L_T + 8192 + m * 4096, lane * 16, so + m * 2048 + 16384 - 2048, 3072);
    }
    wait_until(tstart, TT0 + (4 + (j >> 4)) * TTAU);
#pragma unroll
    for (int m = 0; m < 2; ++m) {
      DMA16(r2, L_T + m * 4096, lane * 16, s2 + m * 4096, 0); DMA16(r2, L_T + m * 4096, lane * 16, s2 + m * 4096, 1024);
      DMA16(r2, L_T + m * 4096, lane * 16, s2 + m * 4096, 2048); DMA16(r2, L_T + m * 4096, lane * 16, s2 + m * 4096, 3072);
    }
    PIN();
    wait_vm<8>(); SETFLAG(FL_T, 4u);
    wait_vm<4>(); SETFLAG(FL_T, 5u);
    wait_vm<0>(); SETFLAG(FL_T, 6u);
    PIN();
    return;
  }

  const int sw = wave - 3, r = lane >> 4, c = lane & 15;
  u32 qspins = 0;
  const int qt = max((int)QTIME, (int)(clk() - tstart) + QD);
  const u32 lb = (u32)(uintptr_t)lds;
  u32 ak0 = lb + L_K + sw * 8192 + c * 256 + ((r ^ c) << 4), ak1 = lb + L_K + sw * 8192 + c * 256 + (((4 + r) ^ c) << 4);
  u32 ak2 = lb + L_K + sw * 8192 + c * 256 + (((8 + r) ^ c) << 4), ak3 = lb + L_K + sw * 8192 + c * 256 + (((12 + r) ^ c) << 4);
  u32 aqw = lb + L_Q + sw * 256 + ((r & 1) * 64 + (r >> 1) * 32 + c) * 2, aqr = lb + L_Q + (c & 3) * 256 + r * 16;
  u32 apw = c < 8 ? lb + L_P + (c & 3) * 320 + sw * 64 + (c & 4) * 8 + r * 8 : lb + L_DUMP + (lane & 31) * 8, apr = lb + L_P + (c >> 2) * 320 + r * 16;
  u32 amw = lane < 4 ? lb + L_M + lane * 16 + sw * 4 : lb + L_DUMP + lane * 4, amr = lb + L_M + r * 16;
  const int q4 = (lane >> 2) & 3, gl = q4 | ((r & 1) << 2);
  u32 vb0 = lb + L_V + (8 * r + q4) * 256 + (((2 * sw) ^ gl) << 5) + (lane & 3) * 8, vb1 = lb + L_V + (8 * r + q4) * 256 + (((2 * sw + 1) ^ gl) << 5) + (lane & 3) * 8;
  u32 gq = (lane >> 5) * 2048 + (lane & 31) * 16, ga = r * 8 * MSG + c * (MSG / 8), go = (lane >> 3) * AROW + (lane & 7) * (MSG / 8), tb = lb + L_T + (sw * 64 + lane) * 4;
  OPAQUE(ak0); OPAQUE(ak1); OPAQUE(ak2); OPAQUE(ak3); OPAQUE(aqw); OPAQUE(aqr); OPAQUE(apw); OPAQUE(apr); OPAQUE(amw); OPAQUE(amr);
  OPAQUE(vb0); OPAQUE(vb1); OPAQUE(gq); OPAQUE(ga); OPAQUE(go); OPAQUE(tb);
  const bool hi = (c & 4) != 0;
  PIN();
  {
    NOSPLIT(2100);
    wait_until(tstart, qt);
    u32x4 qa[2], kf[8];
#pragma unroll
    for (int k = 0; k < 2; ++k) qa[k] = LD16(rs, gq, WS_Q + ((g * 4 + 2 * k) * 512 + sw * 128) * 4);
#define READ_K() do { kf[0] = *LDSA(u32x4, ak0); kf[1] = *LDSA(u32x4, ak1); kf[2] = *LDSA(u32x4, ak2); kf[3] = *LDSA(u32x4, ak3); \
    kf[4] = *LDSA(u32x4, ak0 + 4096); kf[5] = *LDSA(u32x4, ak1 + 4096); kf[6] = *LDSA(u32x4, ak2 + 4096); kf[7] = *LDSA(u32x4, ak3 + 4096); } while (0)
    bool have_k = false;
    for (u32 it = 0; it < LSPIN_CAP; ++it) {
      have_k = ge(peek2(lds, FL_PROG), G_K - 6 + 2 * sw);
      if (have_k || (__builtin_amdgcn_s_getreg(0x1807) & 15u) == 0) break;
    }
    READ_K();
    PIN();
    for (;;) {
      u32 bad = 0;
#pragma unroll
      for (int k = 0; k < 4; ++k) bad |= neq2(qa[0][k], qa[1][k], qtag_of(count) >> (8 * k));
      if (all_zero(bad & 0xffu) || ++qspins > SPIN_CAP) break;
      MEMBAR();
#pragma unroll
      for (int k = 0; k < 2; ++k) qa[k] = LD16(rs, gq, WS_Q + ((g * 4 + 2 * k) * 512 + sw * 128) * 4);
    }
    float qv[4];
#pragma unroll
    for (int k = 0; k < 4; ++k) qv[k] = asf(qa[0][k] & 0xffffff00u) + asf(qa[1][k] & 0xffffff00u);
    const u32 qp = pk_bf16(swap32_add(qv[0], qv[2]), swap32_add(qv[1], qv[3]));
    *LDSA(bf16_t, aqw) = (bf16_t)qp; *LDSA(bf16_t, aqw + 32) = (bf16_t)(qp >> 16);
    lds_signal(lds, FL_S + sw, 1u);
    PIN();
    if (!have_k) { lds_wait_prog(lds, G_K - 6 + 2 * sw); READ_K(); }
    u32x4 qf[4];
    for (u32 it = 0; it < LSPIN_CAP; ++it) {
      const u32x4 f1 = peek4(lds, FL_S);
#pragma unroll
      for (int s = 0; s < 4; ++s) qf[s] = *LDSA(u32x4, aqr + s * 64);
      MEMBAR();
      if (ge(f1, 1u)) break;
    }
    PIN();
    f32x4 s0 = {0.f, 0.f, 0.f, 0.f}, s1 = {0.f, 0.f, 0.f, 0.f};
#pragma unroll
    for (int s = 0; s < 4; ++s) { s0 = mfma(kf[s], qf[s], s0); s1 = mfma(kf[4 + s], qf[s], s1); }
    const u32 a = pk_bf16(hi ? s1[0] : s0[0], hi ? s1[1] : s0[1]), bb = pk_bf16(hi ? s1[2] : s0[2], hi ? s1[3] : s0[3]);
    const u32 a2 = pk_bf16(bflo(a) * 0.08838834764831845f, bfhi(a) * 0.08838834764831845f);
    const u32 b2 = pk_bf16(bflo(bb) * 0.08838834764831845f, bfhi(bb) * 0.08838834764831845f);
    const float t0 = bflo(a2), t1 = bfhi(a2), t2 = bflo(b2), t3 = bfhi(b2);
    float m = vmax(vmax3(t0, t1, t2), t3);
    m = vmax(m, dpp<ROR4>(m)); m = swap16_max(m); m = swap32_max(m);
    *LDSA(float, amw) = m;
    const float p0 = exp_sub(t0, m), p1 = exp_sub(t1, m), p2 = exp_sub(t2, m), p3 = exp_sub(t3, m);
    *LDSA(u32x2, apw) = u32x2{pk_bf16(p0, p1), pk_bf16(p2, p3)};
  }
  lds_signal(lds, FL_S + sw, 2u);
  PIN();
  {
    u32x4 va[3], vb[3], pa[4];
#define READ_V3() do { _Pragma("unroll") for (int k = 0; k < 3; ++k) { va[k] = tr_frag(vb0 + k * 8192, vb0 + k * 8192 + 1024); vb[k] = tr_frag(vb1 + k * 8192, vb1 + k * 8192 + 1024); } } while (0)
    const u32x2 fv = peek2(lds, FL_PROG);
    READ_V3();
    MEMBAR();
    f32x4 mm;
    for (u32 it = 0; it < LSPIN_CAP; ++it) {
      const u32x4 f1 = peek4(lds, FL_S);
      mm = *LDSA(f32x4, amr);
#pragma unroll
      for (int k = 0; k < 4; ++k) pa[k] = *LDSA(u32x4, apr + k * 64);
      MEMBAR();
      if (ge(f1, 2u)) break;
    }
    const float M = vmax(vmax3(mm[0], mm[1], mm[2]), mm[3]);
    float f[4];
#pragma unroll
    for (int k = 0; k < 4; ++k) f[k] = exp_sub(mm[k], M);
    const f32x4 z = {0.f, 0.f, 0.f, 0.f};
    PIN();
    if (!ge(fv, G_V - 2)) { lds_wait_prog(lds, G_V - 2); READ_V3(); }
    float o0 = 0.f, o1 = 0.f;
#pragma unroll
    for (int k = 0; k < 3; ++k) { o0 = fmaf(f[k], mfma(pa[k], va[k], z)[0], o0); o1 = fmaf(f[k], mfma(pa[k], vb[k], z)[0], o1); }
    u32x4 vc, vd;
    for (u32 it = 0; it < LSPIN_CAP; ++it) {
      const u32x2 f0 = peek2(lds, FL_PROG);
      vc = tr_frag(vb0 + 3 * 8192, vb0 + 3 * 8192 + 1024); vd = tr_frag(vb1 + 3 * 8192, vb1 + 3 * 8192 + 1024);
      MEMBAR();
      if (ge(f0, G_V)) break;
    }
    PIN();
    o0 = fmaf(f[3], mfma(pa[3], vc, z)[0], o0); o1 = fmaf(f[3], mfma(pa[3], vd, z)[0], o1);
    ST8((u32x2{pk_bf16(o0, o1), tag}), rs, ga, WS_A + (g * 32 + j) * AROW + sw * 2 * MSG);
  }
  lds_signal(lds, FL_S + sw, 3u);
  PIN();
  NOSPLIT(920);
  {
    u32 wp[24];
#pragma unroll
    for (int m = 0; m < 3; ++m) {
      const int k0 = m == 0 ? 8 : 4 * m - 4, k1 = m == 0 ? 24 : 4 * m;
      u32 tw[16];
      lds_wait1(lds, FL_T, 4 + m);
#pragma unroll
      for (int k = k0; k < k1; ++k) tw[k - k0] = *LDSA(u32, tb + k * 1024);
#pragma unroll
      for (int k = k0; k < k1; k += 4) {
        const int p1 = k < 8 ? 1 : 2, p2 = k < 8 ? 2 : 1;
        wp[k] = perm_lo(tw[k - k0], tw[k - k0 + p1]); wp[k + 1] = perm_hi(tw[k - k0], tw[k - k0 + p1]);
        wp[k + 2] = perm_lo(tw[k - k0 + p2], tw[k - k0 + 3]); wp[k + 3] = perm_hi(tw[k - k0 + p2], tw[k - k0 + 3]);
        KEEP(wp[k]); KEEP(wp[k + 1]); KEEP(wp[k + 2]); KEEP(wp[k + 3]);
      }
      PIN();
    }
    float a0 = 0.f, a1 = 0.f, a2 = 0.f, a3 = 0.f;
    u32x4 oa, ob;
    for (u32 it = 0; it < LSPIN_CAP; ++it) {
      const u32 f0 = peek1(lds, FL_O + 1);
      oa = *LDSP(u32x4, L_OP + 32);
      MEMBAR();
      if (ge(f0, 1u)) break;
    }
#pragma unroll
    for (int k = 0; k < 4; ++k) { a0 = dot2(wp[2 * k], oa[k], a0); a1 = dot2(wp[2 * k + 1], oa[k], a1); }
    KEEP(a0); KEEP(a1);
    PIN();
    for (u32 it = 0; it < LSPIN_CAP; ++it) {
      const u32 f0 = peek1(lds, FL_O);
      oa = *LDSP(u32x4, L_OP); ob = *LDSP(u32x4, L_OP + 16);
      MEMBAR();
      if (ge(f0, 1u)) break;
    }
    PIN();
#pragma unroll
    for (int k = 0; k < 4; ++k) {
      a2 = dot2(wp[8 + 2 * k], oa[k], a2); a3 = dot2(wp[9 + 2 * k], oa[k], a3);
      a0 = dot2(wp[16 + 2 * k], ob[k], a0); a1 = dot2(wp[17 + 2 * k], ob[k], a1);
    }
    ST8((u32x2{pk_bf16(a0 + a2, a1 + a3), tag2}), rs, go, WS_A + (g * 32 + sw * 8) * AROW + j * MSG);
  }
  PIN();
}

extern "C" void kernel_launch(void* const* d_in, const int* in_sizes, int n_in, void* d_out, int out_size,
                              void* d_ws, size_t ws_size, hipStream_t stream) {
  Params p;
  p.x = (const bf16_t*)d_in[0]; p.kc = (const bf16_t*)d_in[1]; p.vc = (const bf16_t*)d_in[2];
  p.wq = (const bf16_t*)d_in[3]; p.wo = (const bf16_t*)d_in[4];
  p.w1 = (const bf16_t*)d_in[6]; p.w3 = (const bf16_t*)d_in[7]; p.w2 = (const bf16_t*)d_in[8];
  p.out = (bf16_t*)d_out; p.ws = (unsigned char*)d_ws;
  hipLaunchKernelGGL(fused_decode, dim3(NWG), dim3(NTHR), 0, stream, p);
}
```
